```python
import torch

def _softmax(x, axis=-1):
    return torch.softmax(x, dim=axis)
def _gelu(x):
    return torch.nn.functional.gelu(x, approximate="tanh")
def _silu(x):
    return torch.nn.functional.silu(x)
def _sigmoid(x):
    return torch.sigmoid(x)
def _layernorm(x, axis=-1, eps=1e-5):
    m = x.mean(dim=axis, keepdim=True)
    v = x.var(dim=axis, keepdim=True, unbiased=False)
    return (x - m) / torch.sqrt(v + eps)
def _scatter_add(tgt, idx, src, axis=0):
    return tgt.clone().index_add_(axis, idx, src)
def _segment_sum(src, seg, n):
    out = torch.zeros((n,) + src.shape[1:], dtype=src.dtype)
    return out.index_add_(0, seg, src)
def _pad(x, pw):
    flat = tuple(v for pair in reversed(pw) for v in pair)
    return torch.nn.functional.pad(x, flat)

kernel_name = 'gated_mix__sliding_block__dense_activation_relu__prompt'

def setup_inputs():

    _w = (lambda a: (lambda t: t.to(torch.bfloat16) if t.is_floating_point() else t)(torch.as_tensor(a)))
    def _A_setup():
        B, S, D = 1, 2048, 640
        return {
            "x": _w((lambda *s: torch.randn(*s, dtype=torch.bfloat16) * 0.05)(B, S, D)),
            "wqkv": _w((lambda *s: torch.randn(*s, dtype=torch.bfloat16) * 1.6)(D, 3 * D)),
            "wo": _w((lambda *s: torch.randn(*s, dtype=torch.bfloat16) * 0.05)(D, D)),
        }
    _A_in = _A_setup()

    def _B_setup():
        B, S, D, Dff = 1, 2048, 640, 1536
        return {
            "x": _w((lambda *s: torch.randn(*s, dtype=torch.bfloat16) * 0.05)(B, S, D)),
            "w1": _w((lambda *s: torch.randn(*s, dtype=torch.bfloat16) * 0.2)(D, Dff)),
            "w2": _w((lambda *s: torch.randn(*s, dtype=torch.bfloat16) * 0.2)(Dff, D)),
        }
    _B_in = _B_setup()

    return {
        "A_x": _A_in["x"],
        "A_wqkv": _A_in["wqkv"],
        "A_wo": _A_in["wo"],
        "B_x": _B_in["x"],
        "B_w1": _B_in["w1"],
        "B_w2": _B_in["w2"],
        "gate_w": _w((lambda *s: torch.randn(*s, dtype=torch.bfloat16) * 0.05)(640) * 16.0),
    }

def reference(A_x, A_wqkv, A_wo, B_x, B_w1, B_w2, gate_w):
    def _A_ref(x, wqkv, wo):
        B, S, D = x.shape
        H = 32; Dh = D // H; W = 256
        qkv = (lambda x, *d: x.permute(*d))((x @ wqkv).reshape(B, S, 3, H, Dh), 2, 0, 3, 1, 4)
        q, k, v = qkv[0], qkv[1], qkv[2]
        scores = torch.einsum("bhqd,bhkd->bhqk", q, k) * (Dh ** -0.5)
        idx = torch.arange(S)
        mask = (idx[:, None] - idx[None, :] >= 0) & (idx[:, None] - idx[None, :] < W)
        scores = torch.where(mask[None, None], scores, -1e9)
        p = _softmax(scores, axis=-1)
        o = torch.einsum("bhqk,bhkd->bhqd", p, v)
        return (lambda x, *d: x.permute(*d))(o, 0, 2, 1, 3).reshape(B, S, D) @ wo

    def _B_ref(x, w1, w2):
        h = torch.einsum("bsd,df->bsf", x, w1)
        zero = torch.zeros_like(h)
        h = torch.maximum(h, zero)
        return torch.einsum("bsf,fd->bsd", h, w2)

    x = A_x
    g = _sigmoid((x * gate_w).sum(-1, keepdims=True))
    a = _A_ref(x, A_wqkv, A_wo)
    b = _B_ref(x, B_w1, B_w2)
    return g * a + (1 - g) * b
```

```cpp
#include <hip/hip_runtime.h>
#include <hip/hip_bf16.h>
typedef unsigned short u16;
__device__ __forceinline__ float bf2f(u16 b){ return __uint_as_float(((unsigned)b)<<16); }
__device__ __forceinline__ u16 f2bf(float f){ __hip_bfloat16 h = __float2bfloat16(f); return *(u16*)&h; }
constexpr int S=2048,D=640,F=1536,H=32,DH=20,W=256;
__global__ void gemm_naive(const u16* A,const u16* B,u16* C,int M,int N,int K,int relu){
  int n=blockIdx.x*blockDim.x+threadIdx.x; int m=blockIdx.y; if(n>=N) return;
  float acc=0; for(int k=0;k<K;k++) acc+=bf2f(A[m*K+k])*bf2f(B[k*N+n]);
  if(relu) acc=fmaxf(acc,0.f);
  C[m*N+n]=f2bf(acc);
}
__global__ void attn_naive(const u16* qkv,u16* o){
  int q=blockIdx.x*blockDim.x+threadIdx.x; int h=blockIdx.y; if(q>=S) return;
  float qv[DH];
  #pragma unroll
  for(int d=0;d<DH;d++) qv[d]=bf2f(qkv[q*3*D+h*DH+d]);
  int k0=q-W+1; if(k0<0)k0=0;
  float m=-1e30f;
  for(int k=k0;k<=q;k++){ float s=0;
  #pragma unroll
    for(int d=0;d<DH;d++) s+=qv[d]*bf2f(qkv[k*3*D+D+h*DH+d]); s*=0.22360679775f; m=fmaxf(m,s);}
  float l=0; float acc[DH];
  #pragma unroll
  for(int d=0;d<DH;d++) acc[d]=0;
  for(int k=k0;k<=q;k++){ float s=0;
  #pragma unroll
    for(int d=0;d<DH;d++) s+=qv[d]*bf2f(qkv[k*3*D+D+h*DH+d]); s*=0.22360679775f; float p=__expf(s-m); l+=p;
  #pragma unroll
    for(int d=0;d<DH;d++) acc[d]+=p*bf2f(qkv[k*3*D+2*D+h*DH+d]); }
  #pragma unroll
  for(int d=0;d<DH;d++) o[q*D+h*DH+d]=f2bf(acc[d]/l);
}
__global__ void combine(const u16* x,const u16* gw,const u16* a,const u16* b,u16* out){
  int m=blockIdx.x; __shared__ float red[64];
  float s=0; for(int k=threadIdx.x;k<D;k+=64) s+=bf2f(x[m*D+k])*bf2f(gw[k]);
  red[threadIdx.x]=s; __syncthreads();
  float t=0; for(int i=0;i<64;i++) t+=red[i];
  float g=1.f/(1.f+__expf(-t));
  for(int k=threadIdx.x;k<D;k+=64) out[m*D+k]=f2bf(g*bf2f(a[m*D+k])+(1.f-g)*bf2f(b[m*D+k]));
}
extern "C" void kernel_launch(void* const* d_in, const int* in_sizes, int n_in, void* d_out, int out_size, void* d_ws, size_t ws_size, hipStream_t stream){
  const u16* x=(const u16*)d_in[0]; const u16* wqkv=(const u16*)d_in[1]; const u16* wo=(const u16*)d_in[2];
  const u16* w1=(const u16*)d_in[4]; const u16* w2=(const u16*)d_in[5]; const u16* gw=(const u16*)d_in[6];
  u16* ws=(u16*)d_ws; u16* qkv=ws; u16* hh=qkv+S*3*D; u16* o=hh+S*F; u16* a=o+S*D; u16* b=a+S*D;
  gemm_naive<<<dim3(3*D/64,S),64,0,stream>>>(x,wqkv,qkv,S,3*D,D,0);
  gemm_naive<<<dim3(F/64,S),64,0,stream>>>(x,w1,hh,S,F,D,1);
  attn_naive<<<dim3(S/64,H),64,0,stream>>>(qkv,o);
  gemm_naive<<<dim3(D/64,S),64,0,stream>>>(o,wo,a,S,D,D,0);
  gemm_naive<<<dim3(D/64,S),64,0,stream>>>(hh,w2,b,S,D,F,0);
  combine<<<S,64,0,stream>>>(x,gw,a,b,(u16*)d_out);
}
```

```cpp
#include <hip/hip_runtime.h>
#include <type_traits>

typedef unsigned short u16;
typedef unsigned int u32;
typedef unsigned long long u64;
typedef short bf16x8 __attribute__((ext_vector_type(8)));
typedef short s16x4 __attribute__((ext_vector_type(4)));
typedef float f32x16 __attribute__((ext_vector_type(16)));
typedef float f32x4 __attribute__((ext_vector_type(4)));
typedef u32 u32x4 __attribute__((ext_vector_type(4)));
typedef u32 u32x2 __attribute__((ext_vector_type(2)));
typedef __amdgpu_buffer_rsrc_t rsrc_t;
#define LDSP(T, a) ((__attribute__((address_space(3))) T*)(a))

#define FUSED 1

constexpr u32 OFF_G = 0, OFF_FQ = 4096, OFF_FH = 8192, OFF_FO = 12288, OFF_GATE = 16384;
constexpr u32 SZ_QKV = 16u * 64u * 2560u;
constexpr u32 OFF_Q = 65536, OFF_K = OFF_Q + SZ_QKV, OFF_V = OFF_K + SZ_QKV;
constexpr u32 OFF_H = OFF_V + SZ_QKV, SZ_H = 32u * 48u * 4096u;
constexpr u32 OFF_O = OFF_H + SZ_H, SZ_O = 32u * 20u * 4096u;
constexpr u32 OFF_WT = OFF_O + SZ_O, SZ_WT = 8u * 68u * 5120u;
constexpr u32 WS_USED = OFF_WT + SZ_WT;

constexpr u32 SLOT = 33792;
constexpr u32 L_GATE = 4 * SLOT;
constexpr u32 L_K = 3 * SLOT;
constexpr u32 L_V = 0;
constexpr u32 L_ONES = 4 * SLOT + 2048;
constexpr u32 L_W3A = SLOT;
constexpr u32 L_W3B = 4 * SLOT + 4096;
constexpr u32 L_CNT = 4 * SLOT + 2048 + 64;
constexpr u32 LDS_BYTES = 163840;
static_assert(L_W3B + 20480 <= LDS_BYTES, "lds");
static_assert(L_W3A + 61440 <= L_K, "lds");

template <int N, class F> __device__ __forceinline__ void sfor_(F&& f, std::integral_constant<int, N>) {}
template <int I, int N, class F> __device__ __forceinline__ void sfor(F&& f) {
  if constexpr (I < N) { f(std::integral_constant<int, I>{}); sfor<I + 1, N>(f); }
}

template <int N> __device__ __forceinline__ void wait_vm() { asm volatile("s_waitcnt vmcnt(%0)" ::"n"(N) : "memory"); }
__device__ __forceinline__ void wait_lgkm0() { asm volatile("s_waitcnt lgkmcnt(0)" ::: "memory"); }
__device__ __forceinline__ void bar() { __builtin_amdgcn_s_barrier(); asm volatile("" ::: "memory"); }

__device__ __forceinline__ void dma(rsrc_t r, u32 voff, u32 soff, u32 ldsdst) {
  soff = __builtin_amdgcn_readfirstlane(soff); ldsdst = __builtin_amdgcn_readfirstlane(ldsdst);
  asm volatile("s_mov_b32 m0, %0\n\ts_nop 3\n\tbuffer_load_dwordx4 %1, %2, %3 offen lds" ::"s"(ldsdst), "v"(voff), "s"(r), "s"(soff) : "memory");
}
__device__ __forceinline__ void dma_sc1(rsrc_t r, u32 voff, u32 soff, u32 ldsdst) {
  soff = __builtin_amdgcn_readfirstlane(soff); ldsdst = __builtin_amdgcn_readfirstlane(ldsdst);
  asm volatile("s_mov_b32 m0, %0\n\ts_nop 3\n\tbuffer_load_dwordx4 %1, %2, %3 offen sc1 lds" ::"s"(ldsdst), "v"(voff), "s"(r), "s"(soff) : "memory");
}
__device__ __forceinline__ bf16x8 lds128(u32 a) { return *LDSP(const bf16x8, a); }
__device__ __forceinline__ s16x4 ldstr(u32 a) { return __builtin_amdgcn_ds_read_tr16_b64_v4i16(LDSP(s16x4, a)); }
__device__ __forceinline__ bf16x8 cat(s16x4 a, s16x4 b) { return __builtin_shufflevector(a, b, 0, 1, 2, 3, 4, 5, 6, 7); }
__device__ __forceinline__ u32 cvtpk(float a, float b) {
  typedef __bf16 bf2 __attribute__((ext_vector_type(2)));
  typedef float f2 __attribute__((ext_vector_type(2)));
  f2 v = {a, b};
  return __builtin_bit_cast(u32, __builtin_convertvector(v, bf2));
}
__device__ __forceinline__ f32x16 mfma32(bf16x8 a, bf16x8 b, f32x16 c) { return __builtin_amdgcn_mfma_f32_32x32x16_bf16(a, b, c, 0, 0, 0); }
__device__ __forceinline__ f32x4 mfma16(bf16x8 a, bf16x8 b, f32x4 c) { return __builtin_amdgcn_mfma_f32_16x16x32_bf16(a, b, c, 0, 0, 0); }
__device__ __forceinline__ void st128(rsrc_t r, u32 voff, u32x4 v) { __builtin_amdgcn_raw_buffer_store_b128(v, r, voff, 0, 16); }
__device__ __forceinline__ bf16x8 ld128(rsrc_t r, u32 voff, u32 soff) { return __builtin_bit_cast(bf16x8, __builtin_amdgcn_raw_buffer_load_b128(r, voff, soff, 16)); }
__device__ __forceinline__ u32x4 pack8(const f32x16& d, int o) {
  u32x4 v = {cvtpk(d[o], d[o + 1]), cvtpk(d[o + 2], d[o + 3]), cvtpk(d[o + 4], d[o + 5]), cvtpk(d[o + 6], d[o + 7])};
  return v;
}

__device__ __forceinline__ void wait_flags(const u64* p, bool active, u64 tag) {
  for (int it = 0; it < (1 << 16); ++it) {
    u64 v = __hip_atomic_load(p, __ATOMIC_RELAXED, __HIP_MEMORY_SCOPE_AGENT);
    bool ok = !active || v == tag;
    if (__all(ok)) break;
    __builtin_amdgcn_s_sleep(2);
  }
  asm volatile("" ::: "memory");
}
__device__ __forceinline__ void set_flag(u64* p, u64 tag) { __hip_atomic_store(p, tag, __ATOMIC_RELAXED, __HIP_MEMORY_SCOPE_AGENT); }

#define STAMP(i) do {} while (0)

constexpr float QSCALE = 0.22360679774997896f * 1.4426950408889634f;
constexpr float NEGBIG = -1e30f;

template <int MASK>
__global__ __launch_bounds__(512, 2) void gm_kernel(const u16* __restrict__ x, const u16* __restrict__ wqkv, const u16* __restrict__ wo,
                                                    const u16* __restrict__ w1, const u16* __restrict__ w2, const u16* __restrict__ gw,
                                                    u16* __restrict__ out, char* __restrict__ ws) {
#if defined(__HIP_DEVICE_COMPILE__)
  constexpr bool SYNC = (MASK == 7);
  const int tid = threadIdx.x, l = tid & 63;
  const int w = __builtin_amdgcn_readfirstlane(tid >> 6);
  const int b = blockIdx.x, xcd = b & 7, bi = b >> 3;
  const int P = 8 * (xcd & 1) + (bi & 7), c = 4 * (xcd >> 1) + (bi >> 3);
  const int h = l >> 5, l31 = l & 31;
  const rsrc_t rws = __builtin_amdgcn_make_buffer_rsrc(ws, (short)0, (int)WS_USED, 0x00020000);
  u64 tag = 0;
  auto load_tag = [&]() {
    if constexpr (SYNC) {
      asm volatile("s_load_dwordx2 %0, %1, 0x0\n\ts_waitcnt lgkmcnt(0)" : "=&s"(tag) : "s"(ws) : "memory");
      tag += 1;
    }
  };
  STAMP(0);

  if constexpr (MASK & 1) {
    const int tg = w & 3, ch = w >> 2;
    const int rot = (P + c) % 10;
    const rsrc_t rx = __builtin_amdgcn_make_buffer_rsrc((void*)x, (short)0, 2048 * 1280, 0x00020000);
    const rsrc_t rw1 = __builtin_amdgcn_make_buffer_rsrc((void*)w1, (short)0, 640 * 3072, 0x00020000);
    bf16x8 xf[40];
    const u32 xr0 = 16 * w + (l >> 3);
    const u32 vx0 = (P * 128 + xr0) * 1280 + (((l & 7) ^ ((xr0 >> 1) & 7)) << 4);
    const u32 vx1 = (vx0 ^ 64) + 8 * 1280;
    const u32 xrow = 32 * tg + l31;
    const u32 xrd = xrow * 128 + ((h ^ ((xrow >> 1) & 7)) << 4);
    u32 vw1[3];
#pragma unroll
    for (int i = 0; i < 3; ++i) { u32 idx = 64 * i + l; vw1[i] = (idx / 12) * 3072 + (idx % 12) * 16 + c * 192; }
    const int tq = (l & 15) >> 2, tp = l & 3, tcol = (l >> 4) & 1;
    auto kchunk = [&](int pos) { int k = pos + rot; return k >= 10 ? k - 10 : k; };
    auto issue_x = [&](int s) {
      const u32 so = kchunk(s) * 128, d = (s & 3) * SLOT + w * 2048;
      dma(rx, vx0, so, d);
      dma(rx, vx1, so, d + 1024);
    };
    auto issue_w1 = [&](int u) {
      const u32 so = (kchunk(2 * u + ch) * 64 + 16 * tg) * 3072, d = ((10 + u) & 3) * SLOT + w * 3072;
      dma(rw1, vw1[0], so, d);
      dma(rw1, vw1[1], so, d + 1024);
      dma(rw1, vw1[2], so, d + 2048);
    };

    if (c < 15) {
      const rsrc_t rwq = __builtin_amdgcn_make_buffer_rsrc((void*)wqkv, (short)0, 640 * 3840, 0x00020000);
      const u32 vwq = (8 * w + (l >> 4)) * 3840 + c * 256 + (((l & 15) ^ (((l >> 4) & 3) << 2)) << 4);
      auto issue = [&](int s) {
        if (s < 10) {
          issue_x(s);
          const u32 so = kchunk(s) * 64 * 3840, d = (s & 3) * SLOT + 16384 + w * 2048;
          dma(rwq, vwq, so, d);
          dma(rwq, vwq, so + 4 * 3840, d + 1024);
        } else issue_w1(s - 10);
      };
      const u32 wrd0 = 16384 + (8 * h + tq) * 256 + (((2 * ch) ^ tq) << 6) + tcol * 32 + tp * 8;
      const u32 wrd1 = wrd0 ^ 64;
      f32x16 acc0 = {}, acc1 = {};
      auto body = [&](auto VT) {
        constexpr bool ISV = decltype(VT)::value;
        issue(0); issue(1); issue(2); issue(3);
        load_tag();
        sfor<0, 10>([&](auto S) {
          constexpr int s = S;
          constexpr int N = s == 0 ? 12 : (s + 1 < 10 ? 4 : 3) + (s + 2 < 10 ? 4 : 3);
          wait_vm<N>(); wait_lgkm0(); bar();
          if (s >= 1) issue(s + 3);
          const u32 sb = (s & 3) * SLOT;
#pragma unroll
          for (int kk = 0; kk < 4; ++kk) {
            xf[4 * s + kk] = lds128(sb + (xrd ^ (kk * 32)));
            bf16x8 a0 = cat(ldstr(sb + wrd0 + kk * 4096), ldstr(sb + wrd0 + kk * 4096 + 1024));
            bf16x8 a1 = cat(ldstr(sb + wrd1 + kk * 4096), ldstr(sb + wrd1 + kk * 4096 + 1024));
            if constexpr (ISV) { acc0 = mfma32(xf[4 * s + kk], a0, acc0); acc1 = mfma32(xf[4 * s + kk], a1, acc1); }
            else { acc0 = mfma32(a0, xf[4 * s + kk], acc0); acc1 = mfma32(a1, xf[4 * s + kk], acc1); }
          }
        });
        STAMP(1);
        const int tb = 4 * P + tg;
        if constexpr (ISV) {
          const int cs = c - 10;
#pragma unroll
          for (int t = 0; t < 2; ++t) {
            const f32x16& d = t ? acc1 : acc0;
            const u32 cv = 128 * cs + 32 * (2 * ch + t) + l31;
            const u32 hp = (cv * 1639) >> 16, c40 = cv - hp * 40;
            const u32 a = OFF_V + (hp * 64 + tb) * 2560 + h * 640 + c40 * 16;
            st128(rws, a, pack8(d, 0));
            st128(rws, a + 1280, pack8(d, 8));
          }
        } else {
          const bool isq = c < 5;
          const int cs = isq ? c : c - 5;
          const float sc = isq ? QSCALE : 1.0f;
          const u32 base = isq ? OFF_Q : OFF_K;
#pragma unroll
          for (int t = 0; t < 2; ++t) {
            f32x16 d = (t ? acc1 : acc0) * sc;
#pragma unroll
            for (int qq = 0; qq < 2; ++qq) {
              u32x4 p = pack8(d, 8 * qq);
              auto r0 = __builtin_amdgcn_permlane32_swap(p[0], p[2], false, false);
              auto r1 = __builtin_amdgcn_permlane32_swap(p[1], p[3], false, false);
              u32x4 v = {r0[0], r1[0], r0[1], r1[1]};
              const u32 G = 16 * cs + 4 * (2 * ch + t) + 2 * qq + h;
              const u32 hp = (G * 205) >> 10, g5 = G - hp * 5;
              st128(rws, base + (hp * 64 + tb) * 2560 + g5 * 512 + l31 * 16, v);
            }
          }
        }
      };
      if (c >= 10) body(std::true_type{}); else body(std::false_type{});
    } else {
      const rsrc_t rw2 = __builtin_amdgcn_make_buffer_rsrc((void*)w2, (short)0, 1536 * 1280, 0x00020000);
      const rsrc_t rwo = __builtin_amdgcn_make_buffer_rsrc((void*)wo, (short)0, 640 * 1280, 0x00020000);
      const rsrc_t rgw = __builtin_amdgcn_make_buffer_rsrc((void*)gw, (short)0, 1280, 0x00020000);
      const rsrc_t rsrc = w < 6 ? rw2 : rwo;
      const u32 vlane = (l >> 3) * 1280 + (((l & 7) ^ (((l >> 4) & 3) << 1)) << 4);
      const u32 rowb = (w < 6 ? 96 * P + 16 * w : 40 * P + 16 * (w - 6)) * 1280;
      auto issue = [&](int s) {
        if (s < 10) {
          const u32 so = kchunk(s) * 128, d = (s & 3) * SLOT + 16384;
          if (w == 0) dma(rwo, vlane, so + (40 * P + 32) * 1280, d + 16384);
          issue_x(s);
          dma(rsrc, vlane, so + rowb, d + w * 2048);
          dma(rsrc, vlane, so + rowb + 8 * 1280, d + w * 2048 + 1024);
        } else issue_w1(s - 10);
      };
      if (w == 1) { dma(rgw, l * 16, 0, L_GATE); dma(rgw, l * 16 + 1024, 0, L_GATE + 1024); }
      const int f = w >> 1, kq = l >> 4;
      const u32 trow = (f < 3 ? 32 * f + 16 * (kq >> 1) : 96 + 20 * (kq >> 1)) + 4 * (kq & 1) + tq;
      const u32 trd0 = 16384 + trow * 128 + (((2 * (w & 1)) ^ ((trow >> 1) & 3)) << 5) + tp * 8;
      const u32 trd1 = trd0 ^ 32;
      const u32 r16 = 112 + tq, r36 = 132 + tq;
      const u32 tl0 = 16384 + r16 * 128 + ((kq ^ ((r16 >> 1) & 3)) << 5) + tp * 8;
      const u32 tl1 = 16384 + r36 * 128 + ((kq ^ ((r36 >> 1) & 3)) << 5) + tp * 8;
      const u32 ksf = f < 3 ? 3 * P + f : 48 + P;
      f32x16 accg = {};
      issue(0); issue(1); issue(2); issue(3);
        load_tag();
      sfor<0, 10>([&](auto S) {
        constexpr int s = S;
        constexpr int N = s == 0 ? 12 : (s + 1 < 10 ? 4 : 3) + (s + 2 < 10 ? 4 : 3) + 2 * (s >= 3 ? 3 : s);
        wait_vm<N>(); wait_lgkm0(); bar();
        if (s >= 1) issue(s + 3);
        const u32 sb = (s & 3) * SLOT;
        const int kc = kchunk(s);
#pragma unroll
        for (int kk = 0; kk < 4; ++kk) {
          xf[4 * s + kk] = lds128(sb + (xrd ^ (kk * 32)));
          if (ch == 0) accg = mfma32(lds128(L_GATE + kc * 128 + kk * 32 + h * 16), xf[4 * s + kk], accg);
        }
#pragma unroll
        for (int t = 0; t < 2; ++t) {
          const u32 a = sb + (t ? trd1 : trd0);
          bf16x8 v = cat(ldstr(a), ldstr(a + 1024));
          const u32 ctg = 4 * kc + 2 * (w & 1) + t, cc = (ctg * 205) >> 10, ct = ctg - cc * 5;
          st128(rws, OFF_WT + ((cc * 68 + ksf) * 5 + ct) * 1024 + l * 16, __builtin_bit_cast(u32x4, v));
        }
        if (w == 7) {
          bf16x8 v = cat(ldstr(sb + tl0), ldstr(sb + tl1));
          const u32 ctg = 4 * kc + kq, cc = (ctg * 205) >> 10, ct = ctg - cc * 5;
          st128(rws, OFF_WT + ((cc * 68 + 64 + (P >> 2)) * 5 + ct) * 1024 + (P & 3) * 256 + (l & 15) * 16, __builtin_bit_cast(u32x4, v));
        }
      });
      STAMP(1);
      float g = 1.0f / (1.0f + __expf(-accg[0]));
      const u32 ga = ch == 0 && h == 0 ? OFF_GATE + (P * 128 + tg * 32 + l31) * 4 : 0xFFFFFFF0u;
      __builtin_amdgcn_raw_buffer_store_b32(__float_as_uint(g), rws, ga, 0, 16);
    }

    {
      const u32 w1rd = (8 * h + tq) * 192 + tcol * 32 + tp * 8 + (ch ? 128 : 0);
      f32x16 acc0 = {}, acc1 = {};
      sfor<0, 5>([&](auto U) {
        constexpr int u = U;
        constexpr int N = u == 0 ? 10 : u == 1 ? 10 : u == 2 ? 6 : u == 3 ? 3 : 0;
        wait_vm<N>(); wait_lgkm0(); bar();
        if (u < 2) issue_w1(u + 3);
        if (SYNC && u == 2 && tid == 0) set_flag((u64*)(ws + OFF_FQ) + P * 16 + c, tag);
        const u32 sb = ((10 + u) & 3) * SLOT + w1rd;
        if (ch == 0) {
#pragma unroll
          for (int kk = 0; kk < 8; ++kk) {
            bf16x8 a0 = cat(ldstr(sb + kk * 3072), ldstr(sb + kk * 3072 + 768));
            bf16x8 a1 = cat(ldstr(sb + kk * 3072 + 64), ldstr(sb + kk * 3072 + 768 + 64));
            acc0 = mfma32(a0, xf[8 * u + kk], acc0);
            acc1 = mfma32(a1, xf[8 * u + kk], acc1);
          }
        } else {
#pragma unroll
          for (int kk = 0; kk < 8; kk += 2) {
            bf16x8 a0 = cat(ldstr(sb + kk * 3072), ldstr(sb + kk * 3072 + 768));
            bf16x8 a1 = cat(ldstr(sb + kk * 3072 + 3072), ldstr(sb + kk * 3072 + 3072 + 768));
            acc0 = mfma32(a0, xf[8 * u + kk], acc0);
            acc1 = mfma32(a1, xf[8 * u + kk + 1], acc1);
          }
        }
      });
      STAMP(2);
      if (ch) acc0 += acc1;
      const u32 R = 2 * P + (tg >> 1), tt = 2 * (tg & 1) + (l31 >> 4);
      const u32 ha = OFF_H + ((R * 48 + 3 * c + 2 * ch) * 4 + tt) * 1024 + h * 256 + (l & 15) * 16;
#pragma unroll
      for (int i = 0; i < 16; ++i) { acc0[i] = fmaxf(acc0[i], 0.f); acc1[i] = fmaxf(acc1[i], 0.f); }
      st128(rws, ha, pack8(acc0, 0));
      st128(rws, ha + 512, pack8(acc0, 8));
      if (ch == 0) {
        st128(rws, ha + 4096, pack8(acc1, 0));
        st128(rws, ha + 4096 + 512, pack8(acc1, 8));
      }
      if constexpr (SYNC) {
        wait_vm<0>(); wait_lgkm0(); bar();
        if (tid == 0) set_flag((u64*)(ws + OFF_FH) + P * 16 + c, tag);
      }
    }
  }
  STAMP(3);

  if constexpr (MASK & 2) {
    const int hp = c, qb = w & 3, hd = w >> 2;
    const int tbq = 4 * P + qb;
    if constexpr (SYNC) {
      const int c0 = (40 * hp) >> 7, c1 = (40 * hp + 39) >> 7;
      const int sec = l < 6 ? 5 : l < 12 ? 10 : 0;
      const int j = l < 6 ? l : l < 12 ? l - 6 : l - 12 + 4;
      const int pp = P - 2 + (j >> 1);
      const int cc = l == 14 ? 15 : sec + ((j & 1) ? c1 : c0);
      const bool act = l < 15 && pp >= 0;
      wait_flags((const u64*)(ws + OFF_FQ) + (act ? (l == 14 ? P : pp) * 16 + cc : 0), act, tag);
    }
    STAMP(4);
    {
      const int i0 = P == 0 ? 20 : P == 1 ? 10 : 0;
      const u32 so = (hp * 64 + 4 * P - 8) * 2560;
#pragma unroll
      for (int i = 0; i < 8; ++i) {
        const int n = w + 8 * i;
        if (n < 60) {
          const int m = n < 30 ? n : n - 30;
          if (m >= i0) dma_sc1(rws, l * 16, (n < 30 ? OFF_K : OFF_V) + so + m * 1024, (n < 30 ? L_K : L_V) + m * 1024);
        }
      }
    }
    const u32 qa = OFF_Q + (hp * 64 + tbq) * 2560;
    bf16x8 q0 = ld128(rws, (hd ? 1536 : 0) + l * 16, qa);
    u32x4 q1r = __builtin_bit_cast(u32x4, ld128(rws, 1024 + l31 * 16, qa));
    const float g = __builtin_bit_cast(float, __builtin_amdgcn_raw_buffer_load_b32(rws, OFF_GATE + (tbq * 32 + l31) * 4, 0, 16));
    if (tid < 2) { u32x4 one = {0x3F803F80u, 0x3F803F80u, 0x3F803F80u, 0x3F803F80u}; *LDSP(u32x4, L_ONES + tid * 1280) = one; }
    if (h) q1r = u32x4{0, 0, 0, 0};
    if (hd) { q1r[0] = 0; q1r[1] = 0; } else { q1r[2] = 0; q1r[3] = 0; }
    const bf16x8 q1 = __builtin_bit_cast(bf16x8, q1r);
    wait_vm<0>(); wait_lgkm0(); bar();
    STAMP(5);

    const u32 k0a = L_K + qb * 2560 + (hd ? 1536 : 0) + l * 16;
    const u32 k1a = L_K + qb * 2560 + 1024 + l31 * 16;
    const bool vdat = l31 < 20;
    const u32 va = vdat ? L_V + qb * 2560 + h * 640 + (20 * hd + l31) * 16 : L_ONES;
    const u32 vstr = vdat ? 2560 : 0;
    auto diag_ok = [&](int r) { return 8 * (r >> 2) + 4 * h + (r & 3) <= l31; };
    const int t0 = tbq >= 8 ? 1 : 8 - tbq;
    const bool has_first = tbq >= 8;
    auto score = [&](int t, f32x16 cin) {
      f32x16 s = mfma32(lds128(k0a + t * 2560), q0, cin);
      return mfma32(lds128(k1a + t * 2560), q1, s);
    };
    auto rmax = [&](float m, const f32x16& s) {
#pragma unroll
      for (int r = 0; r < 16; r += 2) m = fmaxf(fmaxf(m, s[r]), s[r + 1]);
      return m;
    };
    float mx = NEGBIG;
    {
      f32x16 cm;
#pragma unroll
      for (int r = 0; r < 16; ++r) cm[r] = diag_ok(r) ? 0.f : NEGBIG;
      mx = rmax(mx, score(8, cm));
      if (has_first) {
#pragma unroll
        for (int r = 0; r < 16; ++r) cm[r] = diag_ok(r) ? NEGBIG : 0.f;
        mx = rmax(mx, score(0, cm));
#pragma unroll
        for (int t = 1; t < 8; ++t) mx = rmax(mx, score(t, f32x16{}));
      } else {
        for (int t = t0; t < 8; ++t) mx = rmax(mx, score(t, f32x16{}));
      }
      auto rr = __builtin_amdgcn_permlane32_swap(__float_as_uint(mx), __float_as_uint(mx), false, false);
      mx = fmaxf(__uint_as_float(rr[0]), __uint_as_float(rr[1]));
    }
    f32x16 o = {};
    auto pv = [&](int t, f32x16 s) {
#pragma unroll
      for (int r = 0; r < 16; ++r) s[r] = __builtin_amdgcn_exp2f(s[r]);
      const u32 a = va + t * vstr;
      o = mfma32(lds128(a), __builtin_bit_cast(bf16x8, pack8(s, 0)), o);
      o = mfma32(lds128(a + 1280), __builtin_bit_cast(bf16x8, pack8(s, 8)), o);
    };
    {
      f32x16 cm, cn;
#pragma unroll
      for (int r = 0; r < 16; ++r) { cn[r] = -mx; cm[r] = diag_ok(r) ? -mx : NEGBIG; }
      pv(8, score(8, cm));
      if (has_first) {
#pragma unroll
        for (int r = 0; r < 16; ++r) cm[r] = diag_ok(r) ? NEGBIG : -mx;
        pv(0, score(0, cm));
#pragma unroll
        for (int t = 1; t < 8; ++t) pv(t, score(t, cn));
      } else {
        for (int t = t0; t < 8; ++t) pv(t, score(t, cn));
      }
    }
    const float sc = g * __builtin_amdgcn_rcpf(o[12]);
#pragma unroll
    for (int r = 0; r < 12; ++r) o[r] *= sc;
    const u32 R = tbq >> 1, tt = 2 * (tbq & 1) + (l31 >> 4);
    st128(rws, OFF_O + ((R * 20 + hp) * 4 + tt) * 1024 + (2 * hd + h) * 256 + (l & 15) * 16, pack8(o, 0));
    {
      u32x2 tl = {cvtpk(o[8], o[9]), cvtpk(o[10], o[11])};
      const u32 a = h ? 0xFFFFFFF0u : OFF_O + ((R * 20 + 16 + (hp >> 2)) * 4 + tt) * 1024 + (hp & 3) * 256 + (l & 15) * 16 + 8 * hd;
      __builtin_amdgcn_raw_buffer_store_b64(tl, rws, a, 0, 16);
    }
    STAMP(6);
    if constexpr (SYNC) {
      wait_vm<0>(); wait_lgkm0(); bar();
      if (tid == 0) set_flag((u64*)(ws + OFF_FO) + P * 16 + c, tag);
    }
  }
  STAMP(7);

  if constexpr (MASK & 4) {
    const int R = 8 * (xcd & 3) + (bi & 7), cc = 4 * (xcd >> 2) + (bi >> 3);
    const int Pp = R >> 1;
    if constexpr (SYNC) {
      const bool act = l < 18;
      const u64* p = l < 16 ? (const u64*)(ws + OFF_FQ) + l * 16 + 15 : (const u64*)(ws + OFF_FH) + Pp * 16 + 2 * w + (l & 1);
      wait_flags(p, act, tag);
    }
    STAMP(8);
    f32x4 acc[4][5] = {};
    const u32 wb = OFF_WT + cc * 68 * 5120;
    auto step = [&](u32 aoff, u32 ks) {
      bf16x8 af[4], wf[5];
#pragma unroll
      for (int i = 0; i < 4; ++i) af[i] = ld128(rws, l * 16 + i * 1024, aoff);
#pragma unroll
      for (int j = 0; j < 5; ++j) wf[j] = ld128(rws, l * 16 + (j & 3) * 1024, wb + ks * 5120 + (j >> 2) * 4096);
#pragma unroll
      for (int i = 0; i < 4; ++i)
#pragma unroll
        for (int j = 0; j < 5; ++j) acc[i][j] = mfma16(wf[j], af[i], acc[i][j]);
    };
    float gi[4];
#pragma unroll
    for (int i = 0; i < 4; ++i) gi[i] = 1.0f - __builtin_bit_cast(float, __builtin_amdgcn_raw_buffer_load_b32(rws, OFF_GATE + (R * 64 + i * 16 + (l & 15)) * 4, 0, 16));
#pragma unroll
    for (int k = 0; k < 6; ++k) step(OFF_H + (R * 48 + 6 * w + k) * 4096, 6 * w + k);
#pragma unroll
    for (int i = 0; i < 4; ++i)
#pragma unroll
      for (int j = 0; j < 5; ++j) acc[i][j] *= gi[i];
    STAMP(9);
    if constexpr (SYNC) wait_flags((const u64*)(ws + OFF_FO) + Pp * 16 + (l & 15), l < 16, tag);
    STAMP(10);
    step(OFF_O + (R * 20 + w) * 4096, 48 + w);
    step(OFF_O + (R * 20 + 8 + w) * 4096, 56 + w);
    if (w < 4) step(OFF_O + (R * 20 + 16 + w) * 4096, 64 + w);
    STAMP(11);
    wait_lgkm0(); bar();
    {
      const u32 tok = l & 15, q4 = l >> 4;
      const u32 a = w * 20480 + tok * 320 + ((q4 ^ ((tok >> 1) & 3)) << 4);
#pragma unroll
      for (int i = 0; i < 4; ++i)
#pragma unroll
        for (int j = 0; j < 5; ++j) *LDSP(f32x4, a + i * 5120 + j * 64) = acc[i][j];
    }
    wait_lgkm0(); bar();
    const rsrc_t rout = __builtin_amdgcn_make_buffer_rsrc(out, (short)0, 2048 * 1280, 0x00020000);
    auto red = [&](u32 i) {
      f32x4 s = *LDSP(const f32x4, i * 16);
#pragma unroll
      for (int p = 1; p < 8; ++p) s += *LDSP(const f32x4, p * 20480 + i * 16);
      const u32 tok = (i * 3277) >> 16, pos = i - tok * 20, ck = pos ^ ((tok >> 1) & 3);
      u32x2 v = {cvtpk(s[0], s[1]), cvtpk(s[2], s[3])};
      __builtin_amdgcn_raw_buffer_store_b64(v, rout, (R * 64 + tok) * 1280 + cc * 160 + ck * 8, 0, 0);
    };
    red(tid); red(tid + 512);
    if (w < 4) red(tid + 1024);
    if constexpr (SYNC) {
      if (b == 0 && w == 7) {
#pragma unroll
        for (int i = 0; i < 4; ++i) wait_flags((const u64*)(ws + OFF_FH) + i * 64 + l, true, tag);
        if (l == 0) set_flag((u64*)(ws + OFF_G), tag);
      }
    }
  }
  STAMP(12);
#endif
}

extern "C" void kernel_launch(void* const* d_in, const int* in_sizes, int n_in, void* d_out, int out_size, void* d_ws, size_t ws_size,
                              hipStream_t stream) {
  const u16* x = (const u16*)d_in[0];
  const u16* wqkv = (const u16*)d_in[1];
  const u16* wo = (const u16*)d_in[2];
  const u16* w1 = (const u16*)d_in[4];
  const u16* w2 = (const u16*)d_in[5];
  const u16* gw = (const u16*)d_in[6];
  (void)hipFuncSetAttribute((const void*)gm_kernel<7>, hipFuncAttributeMaxDynamicSharedMemorySize, LDS_BYTES);
  gm_kernel<7><<<256, 512, LDS_BYTES, stream>>>(x, wqkv, wo, w1, w2, gw, (u16*)d_out, (char*)d_ws);
}
```

```cpp
#include <hip/hip_runtime.h>
#include <type_traits>

typedef unsigned short u16;
typedef unsigned int u32;
typedef unsigned long long u64;
typedef short bf16x8 __attribute__((ext_vector_type(8)));
typedef short s16x4 __attribute__((ext_vector_type(4)));
typedef float f32x16 __attribute__((ext_vector_type(16)));
typedef float f32x4 __attribute__((ext_vector_type(4)));
typedef u32 u32x4 __attribute__((ext_vector_type(4)));
typedef u32 u32x2 __attribute__((ext_vector_type(2)));
typedef __amdgpu_buffer_rsrc_t rsrc_t;
#define LDSP(T, a) ((__attribute__((address_space(3))) T*)(a))

constexpr u32 OFF_G = 0, OFF_GATE = 16384, OFF_FQ = 32768, OFF_FH = 65536, OFF_FO = 98304;
constexpr int FS = 16;
constexpr u32 SZ_QKV = 16u * 64u * 2560u;
constexpr u32 OFF_Q = 139264, OFF_K = OFF_Q + SZ_QKV, OFF_V = OFF_K + SZ_QKV;
constexpr u32 OFF_H = OFF_V + SZ_QKV;
constexpr u32 OFF_O = OFF_H + 48u * 131072u;
constexpr u32 WS_USED = OFF_O + 20u * 131072u;

constexpr u32 SLOT = 32768;
constexpr u32 L_U[5] = {131072, 65536, 90112, 0, 24576};
constexpr u32 L_K = 65536, L_V = 96256, L_Q = 143360, L_MISC = 159744;
constexpr u32 L_S[5] = {49152, 126976, 0, 16384, 32768};
constexpr u32 L_ONES = L_MISC;
constexpr u32 L_PB = L_MISC + 16, L_PC = L_MISC + 32, L_PD = L_MISC + 48, L_PE = L_MISC + 64, L_PF = L_MISC + 80;
constexpr u32 L_CNT = L_MISC + 96;
constexpr u32 L_ZERO = L_MISC + 112;
constexpr u32 L_GATE = 16384;
constexpr u32 LDS_BYTES = 163840;
enum { C_Q = 0, C_H, C_O };

template <int I, int N, class F> __device__ __forceinline__ void sfor(F&& f) {
  if constexpr (I < N) { f(std::integral_constant<int, I>{}); sfor<I + 1, N>(f); }
}
#define SB() __builtin_amdgcn_sched_barrier(0)
__device__ __forceinline__ u32 opaque(u32 v) { asm volatile("" : "+v"(v)); return v; }

template <int N> __device__ __forceinline__ void wait_vm() { asm volatile("s_waitcnt vmcnt(%0)" ::"n"(N) : "memory"); }
__device__ __forceinline__ void wait_lgkm0() { asm volatile("s_waitcnt lgkmcnt(0)" ::: "memory"); }
__device__ __forceinline__ void bar() { __builtin_amdgcn_s_barrier(); asm volatile("" ::: "memory"); }

__device__ __forceinline__ void setm0(u32 v) {
  v = __builtin_amdgcn_readfirstlane(v);
  asm volatile("s_mov_b32 m0, %0\n\ts_nop 0" ::"s"(v) : "memory");
}
template <int OFF> __device__ __forceinline__ void dma(rsrc_t r, u32 voff, u32 soff) {
  soff = __builtin_amdgcn_readfirstlane(soff);
  asm volatile("buffer_load_dwordx4 %0, %1, %2 offen offset:%3 lds" ::"v"(voff), "s"(r), "s"(soff), "n"(OFF) : "memory");
}
template <int OFF> __device__ __forceinline__ void dma_sc1(rsrc_t r, u32 voff, u32 soff) {
  soff = __builtin_amdgcn_readfirstlane(soff);
  asm volatile("buffer_load_dwordx4 %0, %1, %2 offen offset:%3 sc1 lds" ::"v"(voff), "s"(r), "s"(soff), "n"(OFF) : "memory");
}
template <int OFF> __device__ __forceinline__ bf16x8 ld128_async(rsrc_t r, u32 voff, u32 soff) {
  bf16x8 v;
  soff = __builtin_amdgcn_readfirstlane(soff);
  asm volatile("buffer_load_dwordx4 %0, %1, %2, %3 offen offset:%4 sc1" : "=&v"(v) : "v"(voff), "s"(r), "s"(soff), "n"(OFF) : "memory");
  return v;
}
template <class T> __device__ __forceinline__ void pin(T& v) { asm volatile("" : "+v"(v)); }
__device__ __forceinline__ bf16x8 lds128(u32 a) { return *LDSP(const bf16x8, a); }
__device__ __forceinline__ s16x4 ldstr(u32 a) { return __builtin_amdgcn_ds_read_tr16_b64_v4i16(LDSP(s16x4, a)); }
__device__ __forceinline__ bf16x8 cat(s16x4 a, s16x4 b) { return __builtin_shufflevector(a, b, 0, 1, 2, 3, 4, 5, 6, 7); }
__device__ __forceinline__ u32 cvtpk(float a, float b) {
  typedef __bf16 bf2 __attribute__((ext_vector_type(2)));
  typedef float f2 __attribute__((ext_vector_type(2)));
  f2 v = {a, b};
  return __builtin_bit_cast(u32, __builtin_convertvector(v, bf2));
}
__device__ __forceinline__ f32x16 mfma32(bf16x8 a, bf16x8 b, f32x16 c) { return __builtin_amdgcn_mfma_f32_32x32x16_bf16(a, b, c, 0, 0, 0); }
__device__ __forceinline__ f32x4 mfma16(bf16x8 a, bf16x8 b, f32x4 c) { return __builtin_amdgcn_mfma_f32_16x16x32_bf16(a, b, c, 0, 0, 0); }
__device__ __forceinline__ void st128(rsrc_t r, u32 voff, u32x4 v) { __builtin_amdgcn_raw_buffer_store_b128(v, r, voff, 0, 16); }
__device__ __forceinline__ u32x4 pack8(const f32x16& d, int o) {
  u32x4 v = {cvtpk(d[o], d[o + 1]), cvtpk(d[o + 2], d[o + 3]), cvtpk(d[o + 4], d[o + 5]), cvtpk(d[o + 6], d[o + 7])};
  return v;
}

__device__ __forceinline__ u64 ld_flag(const u64* p) { return __hip_atomic_load(p, __ATOMIC_RELAXED, __HIP_MEMORY_SCOPE_AGENT); }
__device__ __forceinline__ void wait_flags(const u64* p, u64 tag) {
#pragma nounroll
  for (int it = 0; it < (1 << 16); ++it)
    if (__all(ld_flag(p) == tag)) break;
  asm volatile("" ::: "memory");
}
__device__ __forceinline__ void set_flag(u64* p, u64 tag) { __hip_atomic_store(p, tag, __ATOMIC_RELAXED, __HIP_MEMORY_SCOPE_AGENT); }
__device__ __forceinline__ void arrive(int cnt, u32 n, u64* flag, u64 tag, int l) {
  if (l == 0) {
    u32 old = __hip_atomic_fetch_add(LDSP(u32, L_CNT + 4 * cnt), 1u, __ATOMIC_RELAXED, __HIP_MEMORY_SCOPE_WORKGROUP);
    if (old == n - 1) set_flag(flag, tag);
  }
}
__device__ __forceinline__ void prog_set(u32 a, u32 v) {
  asm volatile("s_mov_b64 exec, 1\n\tds_write_b32 %0, %1\n\ts_mov_b64 exec, -1" ::"v"(a), "v"(v) : "memory");
}
__device__ __forceinline__ u32x4 prog_get4(u32 a) { return *LDSP(volatile u32x4, a); }
__device__ __forceinline__ u32 min4(u32x4 v) { return __builtin_amdgcn_readfirstlane(min(min(v[0], v[1]), min(v[2], v[3]))); }
__device__ __forceinline__ void prog_wait4(u32 a, u32 n, u32x4 v) {
  if (min4(v) < n) {
#pragma nounroll
    for (int it = 0; it < (1 << 18); ++it) {
      if (min4(prog_get4(a)) >= n) break;
      __builtin_amdgcn_s_sleep(1);
    }
  }
  asm volatile("" ::: "memory");
}
__device__ __forceinline__ u64 ld_flag_async(const u64* p) {
  u64 v;
  asm volatile("global_load_dwordx2 %0, %1, off sc1" : "=&v"(v) : "v"(p) : "memory");
  return v;
}
__device__ __forceinline__ bool flag_ok(u64 v, u64 tag) { asm volatile("" : "+v"(v)); return __all(v == tag); }

template <u32 OFF> __device__ __forceinline__ void wr_addtid(f32x4 v, u32 m0v) {
  asm volatile("s_mov_b32 m0, %4\n\ts_nop 0\n\tds_write_addtid_b32 %0 offset:%5\n\tds_write_addtid_b32 %1 offset:%5+256\n\t"
               "ds_write_addtid_b32 %2 offset:%5+512\n\tds_write_addtid_b32 %3 offset:%5+768"
               ::"v"(v[0]), "v"(v[1]), "v"(v[2]), "v"(v[3]), "s"(m0v), "n"(OFF) : "memory");
}
__device__ __forceinline__ float ld32_async(rsrc_t r, u32 voff) {
  float v;
  asm volatile("buffer_load_dword %0, %1, %2, 0 offen sc1" : "=&v"(v) : "v"(voff), "s"(r) : "memory");
  return v;
}

#define STAMP(i) do {} while (0)
#define STAMPC(i) do {} while (0)
#define STAMPDUMP() do {} while (0)

constexpr float QSCALE = 0.22360679774997896f * 1.4426950408889634f;
constexpr float NEGBIG = -1e30f;
template <int V> using IC = std::integral_constant<int, V>;

template <int NT, int NREG> struct Sched {
  static constexpr bool wdma(int r) { return r >= NREG && r + 5 <= 16; }
  static constexpr int head(int r) { return (wdma(r) ? 4 : 0) + (r == 9 ? 1 : 0) + (r == 11 ? 3 * NT : 0); }
  static constexpr int anext(int r) { return r <= 8 ? r + 3 : r == 11 || r == 12 ? r + 4 : -1; }
  static constexpr int before(int r) { int n = 3 * NT; for (int q = 0; q < r; ++q) n += head(q) + (anext(q) >= 0 ? NT : 0); return n; }
  static constexpr int aend(int r) {
    return r < 3 ? (r + 1) * NT : r < 12 ? before(r - 2) : r < 15 ? before(11) + head(11) - (14 - r) * NT : before(r - 3);
  }
  static constexpr int wend(int r) { return r >= NREG + 5 && r <= 16 ? before(r - 5) + 4 : 0; }
  static constexpr int need(int r) {
    int e = aend(r) > wend(r + 1) ? aend(r) : wend(r + 1);
    if (r == 11 && before(9) + head(9) > e) e = before(9) + head(9);
    return before(r) - e;
  }
  static constexpr u32 slot(int r) { return r < NREG + 2 ? L_S[r & 1] : r < NREG + 5 ? L_S[r - NREG] : slot(r - 5); }
};

__global__ __launch_bounds__(512, 2) void gm_kernel(const u16* __restrict__ x, const u16* __restrict__ wqkv, const u16* __restrict__ wo,
                                                    const u16* __restrict__ w1, const u16* __restrict__ w2, const u16* __restrict__ gw,
                                                    u16* __restrict__ out, char* __restrict__ ws) {
#if defined(__HIP_DEVICE_COMPILE__)
  asm volatile("" ::"s"(x), "s"(wqkv), "s"(wo), "s"(w1), "s"(w2), "s"(gw), "s"(out), "s"(ws));
  int tid = threadIdx.x, l = tid & 63;
  const int w = __builtin_amdgcn_readfirstlane(tid >> 6);
  const int b = blockIdx.x, xcd = b & 7, bi = b >> 3;
  const int P = 8 * (xcd & 1) + (bi & 7), c = 4 * (xcd >> 1) + (bi >> 3);
  const int Pa = bi & 15, hp = 2 * xcd + (bi >> 4);
  int h = l >> 5, l31 = l & 31;
  auto fresh = [&]() { asm volatile("" : "+v"(tid)); l = tid & 63; h = l >> 5; l31 = l & 31; };
  const int tg = w & 3, ch = w >> 2;
  const rsrc_t rws = __builtin_amdgcn_make_buffer_rsrc(ws, (short)0, (int)WS_USED, 0x00020000);
  u64 tag = 0;
  auto load_tag = [&]() {
    asm volatile("s_load_dwordx2 %0, %1, 0x0\n\ts_waitcnt lgkmcnt(0)" : "=&s"(tag) : "s"(ws) : "memory");
    tag += 1;
  };
  STAMP(0);

#define ROTX ((5 * ((bi ^ (bi >> 3)) & 1) + (xcd >> 1) + 2 * (xcd & 1)) % 10)
  const int rot = ROTX;
  auto kchunk = [&](int pos) { int k = pos + rot; return k >= 10 ? k - 10 : k; };
  const rsrc_t rw1 = __builtin_amdgcn_make_buffer_rsrc((void*)w1, (short)0, 640 * 3072, 0x00020000);
  u32 vw1[3];
  auto mk_vw1 = [&]() {
#pragma unroll
    for (int i = 0; i < 3; ++i) { u32 idx = 64 * i + l; vw1[i] = (idx / 12) * 3072 + (idx % 12) * 16 + c * 192 - i * 1024; }
  };
  auto issue_w1h = [&](int u, int i) {
    const u32 so = (kchunk(2 * u + (tg >> 1)) * 64 + 32 * (tg & 1)) * 3072;
    setm0(L_U[u] + tg * 6144 + i * 3072);
    dma<0>(rw1, vw1[0], so + i * 49152); dma<1024>(rw1, vw1[1], so + i * 49152); dma<2048>(rw1, vw1[2], so + i * 49152);
  };
  auto issue_w1 = [&](int u) { issue_w1h(u, 0); issue_w1h(u, 1); };
  const int i3 = (xcd >> 1) * 32 + bi;
  const int G3 = i3 / 5, cb5 = i3 - G3 * 5, cb = 5 * (xcd & 1) + cb5;
  const int T0 = G3 < 3 ? 6 * G3 : 5 * G3 + 3;
  const bool six = G3 < 3, idle3 = i3 >= 125;
  u64* const fh_own = (u64*)(ws + OFF_FH) + (P * 16 + c) * FS;

  if (ch) {
    u32x4 hpk[6];
    {
      bf16x8 xf[40];
      {
        const rsrc_t rx = __builtin_amdgcn_make_buffer_rsrc((void*)x, (short)0, 2048 * 1280, 0x00020000);
        const rsrc_t rwq = __builtin_amdgcn_make_buffer_rsrc((void*)wqkv, (short)0, 640 * 3840, 0x00020000);
        const u32 vx0 = (P * 128 + 32 * tg + (l >> 3)) * 1280 + (((l & 7) ^ (l >> 4)) << 4);
        const u32 vx1 = (vx0 ^ 64) + 8 * 1280 - 1024;
        const u32 vwq = (16 * tg + (l >> 4)) * 3840 + c * 256 + (((l & 15) ^ (((l >> 4) & 3) << 2)) << 4);
        auto issue_x = [&](int s) {
          const u32 so = kchunk(s) * 128;
          setm0((s & 3) * SLOT + tg * 4096);
          dma<0>(rx, vx0, so); dma<1024>(rx, vx1, so); dma<2048>(rx, vx0, so + 18432); dma<3072>(rx, vx1, so + 18432);
        };
        auto body = [&](auto SP) {
          constexpr bool SPARE = decltype(SP)::value;
          auto issue_wq = [&](int s) {
            if constexpr (!SPARE) {
              const u32 so = kchunk(s) * 64 * 3840;
              setm0((s & 3) * SLOT + 16384 + tg * 4096);
              dma<0>(rwq, vwq, so); dma<1024>(rwq, vwq, so + 14336); dma<2048>(rwq, vwq, so + 28672); dma<3072>(rwq, vwq, so + 43008);
            }
          };
          if constexpr (SPARE) {
            const rsrc_t rgw = __builtin_amdgcn_make_buffer_rsrc((void*)gw, (short)0, 1280, 0x00020000);
            if (tg == 0) { setm0(L_GATE); dma<0>(rgw, l * 16, 0); dma<1024>(rgw, l * 16, 0); }
          }
          STAMP(18);
          issue_x(0); issue_wq(0); issue_x(1); issue_wq(1); issue_x(2);
          STAMP(19);
          load_tag();
          mk_vw1();
          const u32 xrow = 32 * tg + l31;
          const u32 xrd = xrow * 128 + ((h ^ ((xrow >> 1) & 7)) << 4);
          u32 xra[8];
#pragma unroll
          for (int kk = 0; kk < 8; ++kk) xra[kk] = opaque((xrd ^ ((kk & 3) * 32)) + (kk >> 2) * 2 * SLOT);
          sfor<0, 10>([&](auto S) {
            constexpr int s = S;
            wait_vm<(s == 9 ? 12 : (s == 8 ? 10 : 8) + (SPARE ? 0 : 4))>(); wait_lgkm0(); bar();
            if constexpr (s == 0) STAMP(17);
            if constexpr (s + 2 < 10) issue_wq(s + 2);
            SB();
#pragma unroll
            for (int kk = 0; kk < 4; ++kk) xf[4 * s + kk] = lds128((s & 1) * SLOT + xra[(s & 2) * 2 + kk]);
            SB();
            if constexpr (s + 3 < 10) issue_x(s + 3); else issue_w1(s - 7);
          });
        };
        if (c == 15) body(std::true_type{}); else body(std::false_type{});
      }
      STAMP(1);
      wait_vm<12>(); wait_lgkm0(); bar();
      STAMP(2);
      const u32 w1lo = opaque((8 * h + ((l & 15) >> 2)) * 192 + ((l >> 4) & 1) * 32 + (l & 3) * 8);
      const u32 w1hi = opaque(w1lo + 65536), w1h2 = opaque(w1lo + 131072);
      f32x16 hacc[3] = {};
      bf16x8 a[40][3];
      u32x4 cv = {0, 0, 0, 0};
      constexpr int L = 2;
      auto rd = [&](auto J, auto T) {
        constexpr int j = J, t = T;
        if constexpr (j < 40) {
          constexpr int u = j >> 3;
          constexpr u32 o = (L_U[u] & 65535) + (j & 7) * 3072 + t * 64;
          const u32 base = u == 0 ? w1h2 : u < 3 ? w1hi : w1lo;
          a[j][t] = cat(ldstr(base + o), ldstr(base + o + 768));
        }
      };
      sfor<0, L>([&](auto J) { rd(J, IC<0>{}); rd(J, IC<1>{}); rd(J, IC<2>{}); });
      sfor<0, 40>([&](auto M) {
        constexpr int m = M, j = m + L;
        if constexpr (j < 32 && (j & 7) == 0) prog_wait4(L_PB, j >> 3, cv);
        if constexpr (j == 32) prog_wait4(L_PF, 1, cv);
        SB();
        hacc[0] = mfma32(a[m][0], xf[m], hacc[0]); SB();
        rd(IC<j>{}, IC<0>{}); SB();
        hacc[1] = mfma32(a[m][1], xf[m], hacc[1]); SB();
        rd(IC<j>{}, IC<1>{}); SB();
        hacc[2] = mfma32(a[m][2], xf[m], hacc[2]); SB();
        rd(IC<j>{}, IC<2>{});
        if constexpr (m < 2) issue_w1h(3, m);
        if constexpr (m == 1) prog_set(L_PD + 4 * tg, 1);
        if constexpr (m == 3 || m == 9 || m == 17) { wait_vm<12 - 6 * (m >> 3)>(); prog_set(L_PB + 4 * tg, (m >> 3) + 1); }
        if constexpr (j < 24 && (j & 7) == 6) cv = prog_get4(L_PB);
        if constexpr (j == 30) cv = prog_get4(L_PF);
        if constexpr (m == 38) cv = prog_get4(L_PC);
        if constexpr (m == 7 || m == 23 || m == 31) prog_set(L_PC + 4 * tg, m == 7 ? 1 : m == 23 ? 2 : 3);
        SB();
      });
      STAMP(3);
#pragma unroll
      for (int n = 0; n < 6; ++n) {
        const bf16x8 v = __builtin_bit_cast(bf16x8, pack8(hacc[n >> 1], 8 * (n & 1)));
        hpk[n] = __builtin_bit_cast(u32x4, __builtin_elementwise_max(v, bf16x8{}));
      }
      prog_wait4(L_PC, 3, cv);
    }
    fresh();
#pragma unroll
    for (int n = 0; n < 6; ++n) *LDSP(u32x4, L_S[2 + n / 4] + tg * (n < 4 ? 4096 : 2048) + (n & 3) * 1024 + l * 16) = hpk[n];
    wait_lgkm0(); prog_set(L_PC + 4 * tg, 4);
    STAMP(4);
    const int qb = tg, tbq = 4 * Pa + qb;
    f32x16 o[2] = {};
    const u32 ga = opaque(OFF_GATE + (tbq * 32 + l31) * 4);
    const u32 k0a = opaque(L_K + qb * 2560 + l * 16);
    const u32 k1a = opaque(L_K + qb * 2560 + 1024 + l31 * 16);
    const u32 q0a = opaque(k0a - L_K + L_Q), q1a = opaque(h ? L_ZERO : k1a - L_K + L_Q);
    const bool vdat = l31 < 20;
    const u32 va = opaque(vdat ? L_V + qb * 2560 + h * 640 + l31 * 16 : L_ONES);
    const u32 vstr = opaque(vdat ? 320 : 0);
    auto diag_ok = [&](int r) { return 8 * (r >> 2) + 4 * h + (r & 3) <= l31; };
    auto rmax = [&](float m, const f32x16& s) {
#pragma unroll
      for (int r = 0; r < 16; r += 2) m = fmaxf(fmaxf(m, s[r]), s[r + 1]);
      return m;
    };
    auto xmax = [&](float m) {
      auto rr = __builtin_amdgcn_permlane32_swap(__float_as_uint(m), __float_as_uint(m), false, false);
      return fmaxf(__uint_as_float(rr[0]), __uint_as_float(rr[1]));
    };
    auto cfill = [&](int kind, float v) {
      f32x16 cm;
#pragma unroll
      for (int r = 0; r < 16; ++r) cm[r] = kind == 0 ? v : (diag_ok(r) == (kind == 1)) ? v : NEGBIG;
      return cm;
    };
    f32x16 cd[2];
    cd[0] = cfill(1, 0.f); pin(cd[0]);
    cd[1] = cd[0]; pin(cd[1]);
    const u32 vp0 = opaque(va + 64 * vstr);
    prog_wait4(L_PE, 1, prog_get4(L_PE));
    STAMP(8);
    __builtin_amdgcn_s_setprio(1);
    float g = ld32_async(rws, ga);
    bf16x8 q0[2], q1[2];
    bool exact;
    {
      bf16x8 k0[18], k1[9], v0[18], v1[18];
      f32x16 s[18], cm;
      u32x4 qs[2];
      auto rdk = [&](int n) {
        k0[n] = lds128(k0a + (8 - n / 2) * 2560 + (n & 1) * 1536);
        if (!(n & 1)) k1[n / 2] = lds128(k1a + (8 - n / 2) * 2560);
      };
      auto rdv = [&](int n) { const u32 vp = va + ((8 - n / 2) * 8 + (n & 1)) * vstr; v0[n] = lds128(vp); v1[n] = lds128(vp + 1280); };
      q0[0] = lds128(q0a); rdk(0);
      const u32x4 q1r = __builtin_bit_cast(u32x4, lds128(q1a));
      q0[1] = lds128(q0a + 1536); rdk(1);
      SB();
      rdk(2); rdk(3); v0[0] = lds128(vp0); v1[0] = lds128(vp0 + 1280);
      SB();
      q1[0] = __builtin_bit_cast(bf16x8, u32x4{q1r[0], q1r[1], 0, 0});
      q1[1] = __builtin_bit_cast(bf16x8, u32x4{0, 0, q1r[2], q1r[3]});
      s[0] = mfma32(k0[0], q0[0], cd[0]); s[0] = mfma32(k1[0], q1[0], s[0]);
      s[1] = mfma32(k0[1], q0[1], cd[1]); s[1] = mfma32(k1[0], q1[1], s[1]);
      const bf16x8 kone = __builtin_bit_cast(bf16x8, u32x4{h ? 0x3F80u : 0u, 0, 0, 0});
#pragma unroll
      for (int i = 0; i < 2; ++i) {
        const u32 mp = cvtpk(-xmax(rmax(NEGBIG, s[i])), 1.f);
        qs[i] = __builtin_bit_cast(u32x4, q1[i]);
        qs[i][0] = h ? mp : qs[i][0];
        s[i] = mfma32(kone, __builtin_bit_cast(bf16x8, qs[i]), s[i]);
      }
      sfor<0, 18>([&](auto N) {
        constexpr int n = N, u = n + 2, hd = n & 1;
        if constexpr (u + 2 < 18) rdk(u + 2);
        if constexpr (n + 1 < 18) rdv(n + 1);
        if constexpr (u < 18 && hd == 0) {
          u32x4 kk = __builtin_bit_cast(u32x4, k1[u / 2]);
          kk[0] = h ? (tbq >= u / 2 ? 0x3F80u : 0xF1493F80u) : kk[0];
          k1[u / 2] = __builtin_bit_cast(bf16x8, kk);
        }
        if constexpr (u == 16) cm = cfill(2, 0.f);
        f32x16 cin = {};
        if constexpr (u >= 16) cin = cm;
        SB();
        if constexpr (u < 18) s[u] = mfma32(k0[u], q0[hd], cin);
        SB();
#pragma unroll
        for (int r = 0; r < 4; ++r) s[n][r] = __builtin_amdgcn_exp2f(s[n][r]);
        SB();
        if constexpr (u < 18) s[u] = mfma32(k1[u / 2], __builtin_bit_cast(bf16x8, qs[hd]), s[u]);
        SB();
#pragma unroll
        for (int r = 4; r < 8; ++r) s[n][r] = __builtin_amdgcn_exp2f(s[n][r]);
        bf16x8 p0 = __builtin_bit_cast(bf16x8, pack8(s[n], 0));
        SB();
        s[n][8] = __builtin_amdgcn_exp2f(s[n][8]);
        SB();
        o[hd] = mfma32(v0[n], p0, o[hd]);
        SB();
#pragma unroll
        for (int r = 9; r < 16; ++r) s[n][r] = __builtin_amdgcn_exp2f(s[n][r]);
        bf16x8 p1 = __builtin_bit_cast(bf16x8, pack8(s[n], 8));
        SB();
        o[hd] = mfma32(v1[n], p1, o[hd]);
        SB();
      });
      float c0 = o[0][12] * 0x1p63f, c1 = o[1][12] * 0x1p63f;
#pragma unroll
      for (int r = 0; r < 12; ++r) { c0 += fabsf(o[0][r]); c1 += fabsf(o[1][r]); }
      exact = !__all(c0 + c1 < 0x1p127f);
    }
    if (exact) {
      const int t0 = tbq < 8 ? 8 - tbq : 1;
#pragma nounroll
      for (int hd = 0; hd < 2; ++hd) {
        const bf16x8 qa = hd ? q0[1] : q0[0], qc = hd ? q1[1] : q1[0];
        f32x16 oo = {};
        auto score = [&](int t, f32x16 cin) {
          f32x16 s = mfma32(lds128(k0a + t * 2560 + hd * 1536), qa, cin);
          return mfma32(lds128(k1a + t * 2560), qc, s);
        };
        auto pv = [&](int t, f32x16 s) {
#pragma unroll
          for (int r = 0; r < 16; ++r) s[r] = __builtin_amdgcn_exp2f(s[r]);
          const u32 a = va + (t * 8 + hd) * vstr;
          oo = mfma32(lds128(a), __builtin_bit_cast(bf16x8, pack8(s, 0)), oo);
          oo = mfma32(lds128(a + 1280), __builtin_bit_cast(bf16x8, pack8(s, 8)), oo);
        };
        float mx = rmax(NEGBIG, score(8, cfill(1, 0.f)));
        if (tbq >= 8) mx = rmax(mx, score(0, cfill(2, 0.f)));
        for (int t = t0; t < 8; ++t) mx = rmax(mx, score(t, f32x16{}));
        mx = -xmax(mx);
        pv(8, score(8, cfill(1, mx)));
        if (tbq >= 8) pv(0, score(0, cfill(2, mx)));
        const f32x16 cn = cfill(0, mx);
        for (int t = t0; t < 8; ++t) pv(t, score(t, cn));
        if (hd) o[1] = oo; else o[0] = oo;
      }
    }
    STAMP(9);
    {
      fresh();
      wait_vm<0>();
      pin(g);
      const u32 T = 2 * tbq + (l31 >> 4);
      u32x4 tl;
#pragma unroll
      for (int hd = 0; hd < 2; ++hd) {
        const float sc = g * __builtin_amdgcn_rcpf(o[hd][12]);
#pragma unroll
        for (int r = 0; r < 12; ++r) o[hd][r] *= sc;
        st128(rws, OFF_O + (hp * 128 + T) * 1024 + (2 * hd + h) * 256 + (l & 15) * 16, pack8(o[hd], 0));
        tl[2 * hd] = cvtpk(o[hd][8], o[hd][9]); tl[2 * hd + 1] = cvtpk(o[hd][10], o[hd][11]);
      }
      st128(rws, h ? 0xFFFFFFF0u : OFF_O + ((16 + (hp >> 2)) * 128 + T) * 1024 + (hp & 3) * 256 + (l & 15) * 16, tl);
      __builtin_amdgcn_s_setprio(0);
      wait_vm<0>();
      arrive(C_O, 4, (u64*)(ws + OFF_FO) + (Pa * 16 + hp) * FS, tag, l);
    }
    STAMP(10);
    if (idle3) {
      if (i3 == 127 && (xcd & 1) && w == 7) {
#pragma unroll
        for (int i = 0; i < 4; ++i) wait_flags((const u64*)(ws + OFF_FH) + (i * 64 + l) * FS, tag);
        if (l == 0) set_flag((u64*)(ws + OFF_G), tag);
      }
      STAMPDUMP();
      return;
    }
    bar();
  } else {
    if (tid < 2) *LDSP(u32x4, L_ONES + tid * 1280) = u32x4{0x3F803F80u, 0x3F803F80u, 0x3F803F80u, 0x3F803F80u};
    if (tid < 28) *LDSP(u32, L_PB + tid * 4) = 0;
    load_tag();
    if (c < 15) {
      const int th = tg & 1, cf = tg >> 1;
      const u32 xrow = 64 * th + l31;
      const u32 xrd = xrow * 128 + ((h ^ ((xrow >> 1) & 7)) << 4);
      const int tq = (l & 15) >> 2;
      auto body = [&](auto VT) {
        constexpr bool ISV = decltype(VT)::value;
        const u32 c4 = ISV ? (l & 3) : ((l & 1) << 1) | ((l >> 1) & 1);
        const u32 wrd = 16384 + (8 * h + tq) * 256 + (((2 * cf) ^ tq) << 6) + ((l >> 4) & 1) * 32 + c4 * 8;
        u32 xra[8], wra[4];
#pragma unroll
        for (int kk = 0; kk < 8; ++kk) xra[kk] = opaque((xrd ^ ((kk & 3) * 32)) + (kk >> 2) * 2 * SLOT);
#pragma unroll
        for (int kk = 0; kk < 4; ++kk) wra[kk] = opaque((wrd ^ ((kk & 1) * 64)) + (kk >> 1) * 2 * SLOT);
        u32 sa[4];
        if constexpr (ISV) {
#pragma unroll
          for (int t = 0; t < 2; ++t) {
            const u32 cv = 128 * (c - 10) + 32 * (2 * cf + t) + l31;
            const u32 hq = (cv * 1639) >> 16, c40 = cv - hq * 40;
            sa[t] = opaque(OFF_V + (hq * 64 + 4 * P + 2 * th) * 2560 + h * 640 + c40 * 16);
          }
        } else {
          const u32 base = (c < 5 ? OFF_Q : OFF_K) + (4 * P + 2 * th) * 2560 + l31 * 16;
#pragma unroll
          for (int e = 0; e < 4; ++e) {
            const u32 G = 16 * (c < 5 ? c : c - 5) + 8 * cf + 2 * e + h;
            const u32 hq = (G * 205) >> 10, g5 = G - hq * 5;
            sa[e] = opaque(base + hq * 64 * 2560 + g5 * 512);
          }
        }
        f32x16 acc[2][2] = {};
        bf16x8 xa[40][2], wa[40][2];
        constexpr int L = 2;
        auto rdx = [&](auto J, auto T) {
          constexpr int j = J, t = T;
          if constexpr (j < 40) xa[j][t] = lds128(((j >> 2) & 1) * SLOT + t * 4096 + xra[((j >> 3) & 1) * 4 + (j & 3)]);
        };
        auto rdw = [&](auto J, auto T) {
          constexpr int j = J, t = T;
          if constexpr (j < 40) {
            constexpr u32 o = ((j >> 2) & 1) * SLOT + (j & 3) * 4096;
            const u32 base = wra[((j >> 3) & 1) * 2 + t];
            wa[j][t] = cat(ldstr(base + o), ldstr(base + o + 1024));
          }
        };
        auto mm = [&](int m, int i, int t) {
          if constexpr (ISV) acc[i][t] = mfma32(xa[m][i], wa[m][t], acc[i][t]);
          else acc[i][t] = mfma32(wa[m][t], xa[m][i], acc[i][t]);
        };
        wait_lgkm0(); bar();
        STAMP(40); STAMPC(41);
        sfor<0, L>([&](auto J) { rdw(J, IC<0>{}); rdx(J, IC<0>{}); rdx(J, IC<1>{}); rdw(J, IC<1>{}); });
        sfor<0, 40>([&](auto M) {
          constexpr int m = M, j = m + L;
          if constexpr (j == 40) { wait_lgkm0(); STAMP(1); STAMPC(42); bar(); }
          else if constexpr ((j & 3) == 0) { wait_lgkm0(); bar(); }
          SB();
          if constexpr ((j & 3) == 3) {
            mm(m, 0, 0); SB();
            rdw(IC<j>{}, IC<0>{}); rdx(IC<j>{}, IC<0>{}); SB();
            mm(m, 1, 0); SB();
            rdx(IC<j>{}, IC<1>{}); rdw(IC<j>{}, IC<1>{}); SB();
            mm(m, 0, 1); SB();
            mm(m, 1, 1); SB();
          } else {
            mm(m, 0, 0); SB();
            rdw(IC<j>{}, IC<0>{}); SB();
            mm(m, 1, 0); SB();
            rdx(IC<j>{}, IC<0>{}); SB();
            mm(m, 0, 1); SB();
            rdx(IC<j>{}, IC<1>{}); SB();
            mm(m, 1, 1); SB();
            rdw(IC<j>{}, IC<1>{}); SB();
          }
        });
        if constexpr (!ISV) {
          if (c < 5) {
#pragma unroll
            for (int e = 0; e < 4; ++e) acc[e & 1][e >> 1] *= QSCALE;
          }
        }
        u32x4 pk[8];
#pragma unroll
        for (int e = 0; e < 8; ++e) { pk[e] = pack8(acc[(e >> 1) & 1][e >> 2], 8 * (e & 1)); pin(pk[e]); }
        prog_wait4(L_PD, 1, prog_get4(L_PD));
        STAMP(22);
#pragma unroll
        for (int e = 0; e < 8; ++e) {
          const int t = e >> 2, i = (e >> 1) & 1, q = e & 1;
          st128(rws, (ISV ? sa[t] + q * 1280 : sa[2 * t + q]) + i * 2560, pk[e]);
        }
      };
      if (c >= 10) body(std::true_type{}); else body(std::false_type{});
    } else {
      const u32 xrow = 32 * tg + l31;
      const u32 xrd = xrow * 128 + ((h ^ ((xrow >> 1) & 7)) << 4);
      f32x16 accg = {};
      for (int s = 0; s < 10; ++s) {
        wait_lgkm0(); bar();
        const u32 ga = L_GATE + kchunk(s) * 128 + h * 16;
#pragma unroll
        for (int kk = 0; kk < 4; ++kk) accg = mfma32(lds128(ga + kk * 32), lds128((s & 3) * SLOT + (xrd ^ (kk * 32))), accg);
      }
      wait_lgkm0(); STAMP(1); bar();
      const float g = 1.0f / (1.0f + __expf(-accg[0]));
      __builtin_amdgcn_raw_buffer_store_b32(__float_as_uint(g), rws, h == 0 ? OFF_GATE + (P * 128 + tg * 32 + l31) * 4 : 0xFFFFFFF0u, 0, 16);
    }
    STAMP(2);
    fresh();
    auto post = [&](auto NTc) {
      constexpr int NT = NTc, NREG = NT == 6 ? 4 : 6, WN = NT ? 4 : 0;
      using SC = Sched<NT, NREG>;
#define ROT3 (6 * ((cb5 + G3) & 1))
      const int rot3 = ROT3;
      auto ks_of = [&](int r) {
        if (r >= 12) return r < 16 ? 48 + 4 * ((r + cb5) & 3) + tg : 64 + tg;
        int k = r + rot3; k = k >= 12 ? k - 12 : k; return 4 * k + tg;
      };
      const rsrc_t rw2 = __builtin_amdgcn_make_buffer_rsrc((void*)w2, (short)0, 1536 * 1280, 0x00020000);
      const rsrc_t rwo = __builtin_amdgcn_make_buffer_rsrc((void*)wo, (short)0, 640 * 1280, 0x00020000);
      const u32 vw3 = opaque((l >> 3) * 1280 + (((l & 7) ^ (((l >> 4) & 3) << 1)) << 4) + cb * 128);
      const u32 l16 = opaque(l * 16);
      auto issue_w = [&](auto R) {
        constexpr int r = R;
        if constexpr (NT > 0) {
          setm0(SC::slot(r) + tg * 4096);
          if constexpr (r < 12) {
            const u32 so = ks_of(r) * 32 * 1280;
            dma<0>(rw2, vw3, so); dma<1024>(rw2, vw3, so + 9216); dma<2048>(rw2, vw3, so + 18432); dma<3072>(rw2, vw3, so + 27648);
          } else if constexpr (r < 16) {
            const u32 so = (ks_of(r) - 48) * 40 * 1280;
            dma<0>(rwo, vw3, so); dma<1024>(rwo, vw3, so + 9216); dma<2048>(rwo, vw3, so + 23552); dma<3072>(rwo, vw3, so + 32768);
          } else {
            const u32 vwt = vw3 + (l16 >> 9) * (36 * 1280), so = (160 * tg + 16) * 1280;
            dma<0>(rwo, vwt, so); dma<1024>(rwo, vwt, so + 24576); dma<2048>(rwo, vwt, so + 100352); dma<3072>(rwo, vwt, so + 124928);
          }
        }
      };
      bf16x8 wf[17][4];
      u32 wra[4];
      {
        const u32 kq = l >> 4, trow = 16 * (kq >> 1) + 4 * (kq & 1) + ((l & 15) >> 2);
        const u32 trd = tg * 4096 + trow * 128 + (((trow >> 1) & 3) << 5) + (l & 3) * 8;
#pragma unroll
        for (int t = 0; t < 4; ++t) wra[t] = opaque(trd ^ (t * 32));
      }
      auto rdw = [&](auto R, auto T) {
        constexpr int r = R, t = T;
        if constexpr (NT > 0 && r < 17) {
          constexpr u32 sl = SC::slot(r);
          const u32 a = sl > 60000 ? opaque(wra[t] + sl) : wra[t] + sl;
          wf[r][t] = cat(ldstr(a), ldstr(a + 1024));
        }
      };
      auto rdw4 = [&](auto R) { sfor<0, 4>([&](auto T) { rdw(R, T); }); wait_lgkm0(); };
      u64* fq = (u64*)(ws + OFF_FQ);
      mk_vw1();
      wait_vm<0>();
      arrive(C_Q, 4, fq + (P * 16 + c) * FS, tag, l);
      STAMP(4);
      issue_w1(4);
      issue_w(IC<0>{});
      prog_wait4(L_PC, 1, prog_get4(L_PC));
      issue_w(IC<1>{});
      STAMP(3);
      wait_vm<2 * WN>(); prog_set(L_PF + 4 * tg, 1);
      STAMP(24);
      wait_vm<WN>(); rdw4(IC<0>{}); issue_w(IC<2>{});
      wait_vm<WN>(); rdw4(IC<1>{}); issue_w(IC<3>{});
      STAMP(20);
      int hst = 0;
      auto h_ready = [&]() { return __builtin_amdgcn_readfirstlane(*LDSP(volatile u32, L_PC + 4 * tg)) >= 4; };
      auto h_store = [&]() {
        asm volatile("" ::: "memory");
        const u32 ha = OFF_H + (3 * c * 128 + 8 * P + 2 * tg + (l31 >> 4)) * 1024 + h * 256 + (l & 15) * 16;
        u32x4 hv[6];
#pragma unroll
        for (int n = 0; n < 6; ++n) hv[n] = *LDSP(const u32x4, L_S[2 + n / 4] + tg * (n < 4 ? 4096 : 2048) + (n & 3) * 1024 + l16);
#pragma unroll
        for (int n = 0; n < 6; ++n) st128(rws, ha + (n >> 1) * 131072 + (n & 1) * 512, hv[n]);
        prog_set(L_PF + 4 * tg, 2);
      };
      {
        const int c0 = (40 * hp) >> 7, c1 = (40 * hp + 39) >> 7;
        const int pp = tg < 3 ? Pa - 2 + tg : Pa;
        if (pp >= 0) {
          int idx = pp * 16 + (tg == 3 ? 0 : (l & 2) ? 10 : 5) + ((l & 1) ? c1 : c0);
          if (tg == 3 && (l & 2)) idx = pp * 16 + 15;
#pragma nounroll
          for (int it = 0; it < (1 << 16); ++it) {
            const bool ok = __all(ld_flag(fq + idx * FS) == tag);
            if (hst == 1) { wait_vm<0>(); arrive(C_H, 4, fh_own, tag, l); hst = 2; STAMP(23); }
            if (ok) break;
            if (hst == 0 && h_ready()) { h_store(); hst = 1; }
          }
          asm volatile("" ::: "memory");
          STAMP(5);
          const u32 so = (hp * 64 + 4 * pp) * 2560;
          auto ten = [&](u32 s, u32 d) {
            setm0(d); dma_sc1<0>(rws, l16, s); dma_sc1<1024>(rws, l16, s); dma_sc1<2048>(rws, l16, s); dma_sc1<3072>(rws, l16, s);
            setm0(d + 4096); dma_sc1<0>(rws, l16, s + 4096); dma_sc1<1024>(rws, l16, s + 4096); dma_sc1<2048>(rws, l16, s + 4096); dma_sc1<3072>(rws, l16, s + 4096);
            setm0(d + 8192); dma_sc1<0>(rws, l16, s + 8192); dma_sc1<1024>(rws, l16, s + 8192);
          };
          if (tg == 3) ten(OFF_Q + so, L_Q);
          else {
            prog_wait4(L_PC, 2, prog_get4(L_PC));
            ten(OFF_K + so, L_K + tg * 10240); ten(OFF_V + so, L_V + tg * 10240);
          }
        } else {
          wait_vm<0>();
          prog_wait4(L_PC, 2, prog_get4(L_PC));
#pragma unroll
          for (int i = 0; i < 10; ++i) {
            *LDSP(u32x4, L_K + tg * 10240 + i * 1024 + l16) = u32x4{0, 0, 0, 0};
            *LDSP(u32x4, L_V + tg * 10240 + i * 1024 + l16) = u32x4{0, 0, 0, 0};
          }
        }
        STAMP(6);
      }
      if (hst == 0 && h_ready()) {
        h_store();
        wait_vm<6>(); wait_lgkm0(); prog_set(L_PE + 4 * tg, 1);
        hst = 1;
      } else {
        wait_vm<0>(); wait_lgkm0(); prog_set(L_PE + 4 * tg, 1);
      }
      STAMP(7);
      if (hst == 0) {
#pragma nounroll
        for (int it = 0; it < (1 << 18); ++it) {
          if (h_ready()) break;
          __builtin_amdgcn_s_sleep(1);
        }
        h_store();
        hst = 1;
      }
      if (hst == 1) { wait_vm<0>(); arrive(C_H, 4, fh_own, tag, l); STAMP(23); }
      if constexpr (NT == 0) { STAMPDUMP(); return; } else {
        rdw4(IC<2>{}); issue_w(IC<4>{});
        rdw4(IC<3>{}); issue_w(IC<5>{});
        issue_w(IC<NREG + 2>{});
        prog_wait4(L_PF, 2, prog_get4(L_PF));
        issue_w(IC<NREG + 3>{}); issue_w(IC<NREG + 4>{});
        if constexpr (NREG == 6) {
          wait_vm<12>(); rdw4(IC<4>{}); issue_w(IC<6>{}); rdw4(IC<5>{}); issue_w(IC<7>{});
        }
        STAMP(8);
        const int fidx = ((T0 + ((l & 16) ? NT - 1 : 0)) >> 3) * 16 + (l & 15);
        wait_flags((const u64*)(ws + OFF_FH) + fidx * FS, tag);
        STAMP(11);
        const u32 lg = L_MISC + 1536 + tg * 384;
        setm0(lg);
        if (l < 4 * NT) dma_sc1<0>(rws, l16, OFF_GATE + T0 * 64);
        f32x4 acc[NT][4] = {};
        bf16x8 af[17][NT];
        auto lda = [&](auto R, auto I) {
          constexpr int r = R, i = I;
          af[r][i] = ld128_async<(i & 3) * 1024>(rws, l16, OFF_H + (ks_of(r) * 128 + T0) * 1024 + (i >> 2) * 4096);
        };
        auto lda_all = [&](auto R) { sfor<0, NT>([&](auto I) { lda(R, I); }); };
        lda_all(IC<0>{}); lda_all(IC<1>{}); lda_all(IC<2>{});
        u64 fov = 0;
        auto wr_part = [&](auto I, auto T) {
          constexpr int i = I, t = T;
          constexpr u32 CTS = 4 * NT * 1024 + 64, off = t * CTS + i * 1024 - (t >= 2 ? 32768 : 0);
          wr_addtid<off>(acc[i][t], tg * NT * 1024 + (t >= 2 ? 32768 : 0));
        };
        sfor<0, 17>([&](auto R) {
          constexpr int r = R;
          wait_vm<SC::need(r)>(); wait_lgkm0();
          if constexpr (r == 16) bar();
          sfor<0, NT>([&](auto I) { pin(af[r][I]); });
          if constexpr (SC::wdma(r)) issue_w(IC<r + 5>{});
          if constexpr (r == 9 || r == 11) {
            const u64* fo = (const u64*)(ws + OFF_FO) + (((T0 + ((l16 & 256) ? NT - 1 : 0)) >> 3) * 16 + ((l16 >> 4) & 15)) * FS;
            if constexpr (r == 9) fov = ld_flag_async(fo);
            else {
              STAMP(12);
              if (!flag_ok(fov, tag)) wait_flags(fo, tag);
              STAMP(21);
              lda_all(IC<12>{}); lda_all(IC<13>{}); lda_all(IC<14>{});
            }
          }
          SB();
          sfor<0, NT * 4>([&](auto E) {
            constexpr int e = E, i = e >> 2, t = e & 3;
            acc[i][t] = mfma16(af[r][i], wf[r][t], acc[i][t]);
            SB();
            if constexpr (r + 1 >= NREG && e < 8 && (e & 1)) rdw(IC<r + 1>{}, IC<e / 2>{});
            if constexpr (r == 16 && i > 0) wr_part(IC<i - 1>{}, IC<t>{});
            if constexpr (t == 3 && SC::anext(r) >= 0) lda(IC<(SC::anext(r) >= 0 ? SC::anext(r) : 0)>{}, IC<i>{});
            SB();
          });
          if constexpr (r == 11) {
#pragma unroll
            for (int i = 0; i < NT; ++i) {
              const f32x4 gi = 1.0f - *LDSP(const f32x4, lg + i * 64 + (l16 >> 8) * 16);
#pragma unroll
              for (int t = 0; t < 4; ++t) acc[i][t] *= gi;
            }
          }
        });
        STAMP(13);
        asm volatile("s_nop 7\n\ts_nop 7" ::: "memory");
        sfor<0, 4>([&](auto T) { wr_part(IC<NT - 1>{}, T); });
      }
    };
    if (idle3) { post(IC<0>{}); return; }
    if (six) post(IC<6>{}); else post(IC<5>{});
  }
  fresh();
  {
    const int NT = six ? 6 : 5;
    const u32 CTS = 4 * NT * 1024 + 64;
    const rsrc_t rout = __builtin_amdgcn_make_buffer_rsrc(out, (short)0, 2048 * 1280, 0x00020000);
    const u32 u = 2 * (w ^ 4) + h, i = u >> 2, tq = u & 3, j = (l >> 3) & 3, ct = (l >> 1) & 3, hf = l & 1, sw = j & 1;
    const u32 a = ct * CTS + (i * 4 + j) * 256 + tq * 64 + hf * 32;
    u32 a0[4], a1[4];
#pragma unroll
    for (int p = 0; p < 4; ++p) { a0[p] = opaque(a + sw * 16 + p * NT * 1024); a1[p] = opaque(a + 16 - sw * 16 + p * NT * 1024); }
    const u32 oa = opaque(((T0 + i) * 16 + 4 * tq + j) * 1280 + cb * 128 + ct * 32 + hf * 16);
    wait_lgkm0(); bar();
    STAMP(14);
    auto red = [&](auto K) {
      f32x4 s0[K], s1[K];
#pragma unroll
      for (int k = 0; k < K; ++k) {
        s0[k] = *LDSP(const f32x4, a0[0] + k * 4096); s1[k] = *LDSP(const f32x4, a1[0] + k * 4096);
#pragma unroll
        for (int p = 1; p < 4; ++p) { s0[k] += *LDSP(const f32x4, a0[p] + k * 4096); s1[k] += *LDSP(const f32x4, a1[p] + k * 4096); }
      }
#pragma unroll
      for (int k = 0; k < K; ++k) {
        const u32 p0 = cvtpk(s0[k][0], s0[k][1]), p1 = cvtpk(s0[k][2], s0[k][3]), p2 = cvtpk(s1[k][0], s1[k][1]), p3 = cvtpk(s1[k][2], s1[k][3]);
        u32x4 v = sw ? u32x4{p2, p3, p0, p1} : u32x4{p0, p1, p2, p3};
        __builtin_amdgcn_raw_buffer_store_b128(v, rout, oa + k * 64 * 1280, 0, 16);
      }
    };
    if ((w ^ 4) < 2 * NT - 8) red(IC<2>{}); else red(IC<1>{});
  }
  STAMP(15);
  STAMPDUMP();
#endif
}

extern "C" void kernel_launch(void* const* d_in, const int* in_sizes, int n_in, void* d_out, int out_size, void* d_ws, size_t ws_size,
                              hipStream_t stream) {
  const u16* x = (const u16*)d_in[0];
  const u16* wqkv = (const u16*)d_in[1];
  const u16* wo = (const u16*)d_in[2];
  const u16* w1 = (const u16*)d_in[4];
  const u16* w2 = (const u16*)d_in[5];
  const u16* gw = (const u16*)d_in[6];
  (void)hipFuncSetAttribute((const void*)gm_kernel, hipFuncAttributeMaxDynamicSharedMemorySize, LDS_BYTES);
  gm_kernel<<<256, 512, LDS_BYTES, stream>>>(x, wqkv, wo, w1, w2, gw, (u16*)d_out, (char*)d_ws);
}
```
